```python
import torch

def _softmax(x, axis=-1):
    return torch.softmax(x, dim=axis)
def _gelu(x):
    return torch.nn.functional.gelu(x, approximate="tanh")
def _silu(x):
    return torch.nn.functional.silu(x)
def _sigmoid(x):
    return torch.sigmoid(x)
def _layernorm(x, axis=-1, eps=1e-5):
    m = x.mean(dim=axis, keepdim=True)
    v = x.var(dim=axis, keepdim=True, unbiased=False)
    return (x - m) / torch.sqrt(v + eps)
def _scatter_add(tgt, idx, src, axis=0):
    return tgt.clone().index_add_(axis, idx, src)
def _segment_sum(src, seg, n):
    out = torch.zeros((n,) + src.shape[1:], dtype=src.dtype)
    return out.index_add_(0, seg, src)
def _pad(x, pw):
    flat = tuple(v for pair in reversed(pw) for v in pair)
    return torch.nn.functional.pad(x, flat)

kernel_name = 'standalone__linear_kernel_rfa__prompt'

def setup_inputs():

    _w = (lambda a: (lambda t: t.to(torch.bfloat16) if t.is_floating_point() else t)(torch.as_tensor(a)))
    B, H, Sq, Sk, D, M = 1, 32, 2048, 2048, 192, 64
    return {
        "q": _w((lambda *s: torch.randn(*s, dtype=torch.bfloat16) * 0.1)(B, H, Sq, D)),
        "k": _w((lambda *s: torch.randn(*s, dtype=torch.bfloat16) * 1.6)(B, H, Sk, D)),
        "v": _w((lambda *s: torch.randn(*s, dtype=torch.bfloat16) * 1.6)(B, H, Sk, D)),
        "omega": _w((lambda *s: torch.randn(*s, dtype=torch.bfloat16) * 1.6)(M, D)),
    }

def reference(q, k, v, omega):
    d = q.shape[-1]
    m = omega.shape[0]
    scale = float(d) ** -0.25
    qs = q * scale
    ks = k * scale
    q_proj = torch.einsum("bhsd,md->bhsm", qs, omega)
    k_proj = torch.einsum("bhsd,md->bhsm", ks, omega)
    q_sq = 0.5 * torch.einsum("bhsd,bhsd->bhs", qs, qs)[..., None]
    k_sq = 0.5 * torch.einsum("bhsd,bhsd->bhs", ks, ks)[..., None]
    inv_sqrt_m = float(m) ** -0.5
    phi_q = torch.exp(q_proj - q_sq) * inv_sqrt_m + 1e-6
    phi_k = torch.exp(k_proj - k_sq) * inv_sqrt_m + 1e-6
    kv = torch.einsum("bhsm,bhsd->bhmd", phi_k, v)
    z  = torch.einsum("bhsm->bhm", phi_k)
    num = torch.einsum("bhqm,bhmd->bhqd", phi_q, kv)
    den = torch.einsum("bhqm,bhm->bhq", phi_q, z)[..., None] + 1e-6
    return num / den
```

```cpp
#include <hip/hip_runtime.h>
#include <hip/hip_bf16.h>
#include <stdint.h>

typedef __attribute__((ext_vector_type(8))) __bf16 bf16x8;
typedef __attribute__((ext_vector_type(4))) short s16x4;
typedef __attribute__((ext_vector_type(8))) short s16x8;
typedef __attribute__((ext_vector_type(4))) float f32x4;
typedef __attribute__((ext_vector_type(2))) float f32x2;
typedef __attribute__((ext_vector_type(2))) __bf16 bf16x2;
typedef __attribute__((ext_vector_type(4))) unsigned u32x4;
typedef __attribute__((ext_vector_type(2))) unsigned u32x2;
typedef unsigned long long u64;

#define MFMA16(a, b, c) __builtin_amdgcn_mfma_f32_16x16x32_bf16((a), (b), (c), 0, 0, 0)
#define PIN() __builtin_amdgcn_sched_barrier(0)

constexpr int Hh = 32, Ss = 2048, Dd = 192, Mm = 64;
constexpr int ROWS = 256;

constexpr unsigned WS_PART = 0;
constexpr unsigned WS_ZPART = WS_PART + 256u * 24576u;
constexpr unsigned WS_KVF = WS_ZPART + 256u * 256u;
constexpr unsigned WS_F1 = WS_KVF + 32u * 24576u;
constexpr unsigned WS_F2 = WS_F1 + 256u * 64u;
constexpr unsigned WS_EP = WS_F2 + 256u * 64u;
constexpr unsigned WS_END = WS_EP + 32u * 64u;

static __device__ __forceinline__ float bf_lo(unsigned u) { return __uint_as_float(u << 16); }
static __device__ __forceinline__ float bf_hi(unsigned u) { return __uint_as_float(u & 0xffff0000u); }
static __device__ __forceinline__ unsigned pk2(float a, float b) {
    f32x2 f = {a, b};
    return __builtin_bit_cast(unsigned, __builtin_convertvector(f, bf16x2));
}
static __device__ __forceinline__ float rb1(float a) { return bf_lo(pk2(a, a)); }

static __device__ __forceinline__ float dot2acc(unsigned p, float acc) {
#if defined(__HIP_DEVICE_COMPILE__)
    bf16x2 b = __builtin_bit_cast(bf16x2, p);
    return __builtin_amdgcn_fdot2_f32_bf16(b, b, acc, false);
#else
    return acc;
#endif
}

static __device__ __forceinline__ u32x4 scale8(u32x4 in, float scale, float& sq) {
    u32x4 o;
#pragma unroll
    for (int i = 0; i < 4; ++i) {
        unsigned p = pk2(bf_lo(in[i]) * scale, bf_hi(in[i]) * scale);
        sq = dot2acc(p, sq);
        o[i] = p;
    }
    return o;
}

static __device__ __forceinline__ float fexp(float x) { return __builtin_amdgcn_exp2f(x * 1.44269504088896341f); }

static __device__ __forceinline__ unsigned phi2(float p0, float p1, float s0, float s1) {
    unsigned pp = pk2(p0, p1);
    unsigned tt = pk2(bf_lo(pp) - s0, bf_hi(pp) - s1);
    unsigned ee = pk2(fexp(bf_lo(tt)), fexp(bf_hi(tt)));
    return pk2(fmaf(bf_lo(ee), 0.125f, 1e-6f), fmaf(bf_hi(ee), 0.125f, 1e-6f));
}

static __device__ __forceinline__ bf16x8 ld_frag(const char* p) { return *(const bf16x8*)p; }
static __device__ __forceinline__ bf16x8 as_frag(u32x4 v) { return __builtin_bit_cast(bf16x8, v); }
static __device__ __forceinline__ u32x4 ldnt(const void* p) { return __builtin_nontemporal_load((const u32x4*)p); }

static __device__ __forceinline__ bf16x8 ld_tr(const char* p0, const char* p1) {
    typedef __attribute__((address_space(3))) s16x4 lds_s16x4;
    s16x4 lo = __builtin_amdgcn_ds_read_tr16_b64_v4i16((lds_s16x4*)(p0));
    s16x4 hi = __builtin_amdgcn_ds_read_tr16_b64_v4i16((lds_s16x4*)(p1));
    s16x8 r = __builtin_shufflevector(lo, hi, 0, 1, 2, 3, 4, 5, 6, 7);
    return __builtin_bit_cast(bf16x8, r);
}

static __device__ __forceinline__ void stage_omega(char* omf, const u32x4* omr, int t) {
    const int m = t >> 3, c8 = t & 7;
#pragma unroll
    for (int i = 0; i < 3; ++i) {
        int c = c8 + 8 * i;
        int kk = c >> 2, g = c & 3;
        *(u32x4*)(omf + ((((m >> 4) * 6 + kk) * 64) + g * 16 + (m & 15)) * 16) = omr[i];
    }
}

static __device__ __forceinline__ void wait_flags(const char* ws, unsigned off, int head, u64 tag, int w, int l) {
    if (w == 0) {
        const u64* fp = (const u64*)(ws + off + (unsigned)(head * 8 + (l & 7)) * 64u);
        bool ok = (l >= 8);
        for (int spin = 0; spin < (1 << 16); ++spin) {
            if (!ok) ok = (__hip_atomic_load(fp, __ATOMIC_RELAXED, __HIP_MEMORY_SCOPE_AGENT) == tag);
            if (__all(ok)) break;
            __builtin_amdgcn_s_sleep(1);
        }
    }
    __syncthreads();
}

constexpr int L_VS = 0;
constexpr int L_PHIF = L_VS + ROWS * 384;
constexpr int L_OMF = L_PHIF + 8 * 4 * 1024;
constexpr int L_END = L_OMF + 24 * 1024;
constexpr int L_KVF = L_VS;
constexpr int L_ZF = L_VS + 24 * 1024;
constexpr int L_OST = L_VS + 25 * 1024;

__global__ __launch_bounds__(576, 2) void rfa_fused(const uint16_t* __restrict__ Qg, const uint16_t* __restrict__ Kg,
                                                    const uint16_t* __restrict__ Vg, const uint16_t* __restrict__ OM,
                                                    uint16_t* __restrict__ Og, char* __restrict__ ws, float scale) {
    __shared__ __attribute__((aligned(16))) char smem[L_END];
    char* Vs = smem + L_VS;
    char* phiF = smem + L_PHIF;
    char* omF = smem + L_OMF;
    char* kvF = smem + L_KVF;
    char* zF = smem + L_ZF;

    const int t = threadIdx.x, w = t >> 6, l = t & 63, l15 = l & 15, g4 = l >> 4;
    const int b = blockIdx.x;
    const int head = (b & 7) * 4 + ((b >> 3) & 3);
    const int chunk = b >> 5;
    const int wg = head * 8 + chunk;
    const size_t base = ((size_t)head * Ss + (size_t)chunk * ROWS) * Dd;
    const int row = t >> 3, c8 = t & 7;
    const __amdgpu_buffer_rsrc_t wsr = __builtin_amdgcn_make_buffer_rsrc(ws, (short)0, (int)WS_END, 0x00020000);

    if (w == 8) {
        typedef __attribute__((address_space(1))) const void gvoid;
        typedef __attribute__((address_space(3))) void lvoid;
        const char* vbase = (const char*)(Vg + base);
        unsigned offb[3];
#pragma unroll
        for (int bb = 0; bb < 3; ++bb) {
            int P = 64 * bb + l;
            int rowb = P / 24, pc = P % 24;
            int c = (pc & ~7) | ((pc & 7) ^ (rowb & 6));
            offb[bb] = (unsigned)(rowb * 384 + c * 16);
        }
        __builtin_amdgcn_s_barrier();
#pragma unroll
        for (int a = 0; a < 32; ++a)
#pragma unroll
            for (int bb = 0; bb < 3; ++bb)
                __builtin_amdgcn_global_load_lds((gvoid*)(vbase + a * (8 * 384) + offb[bb]),
                                                 (lvoid*)(Vs + (3 * a + bb) * 1024), 16, 0, 2);
        asm volatile("s_waitcnt vmcnt(63)" ::: "memory");
        __builtin_amdgcn_s_barrier();
        asm volatile("s_waitcnt vmcnt(48)" ::: "memory");
        __builtin_amdgcn_s_barrier();
        asm volatile("s_waitcnt vmcnt(24)" ::: "memory");
        __builtin_amdgcn_s_barrier();
        asm volatile("s_waitcnt vmcnt(0)" ::: "memory");
        __builtin_amdgcn_s_barrier();
#pragma unroll
        for (int i = 0; i < 5; ++i) __builtin_amdgcn_s_barrier();
        return;
    }

    const u64 tag = *(const volatile u64*)(ws + WS_EP + (unsigned)head * 64u) + 1ull;

    u32x4 omr[3];
#pragma unroll
    for (int i = 0; i < 3; ++i) omr[i] = ((const u32x4*)OM)[row * 24 + c8 + 8 * i];
    const char* kbase = (const char*)(Kg + base) + (32 * w + l15) * 384 + g4 * 16;
    u32x4 kf[2][6];
#pragma unroll
    for (int kk = 0; kk < 6; ++kk) kf[0][kk] = ldnt(kbase + kk * 64);
    PIN();
    stage_omega(omF, omr, t);

    __syncthreads();

#pragma unroll
    for (int kk = 0; kk < 6; ++kk) kf[1][kk] = ldnt(kbase + 16 * 384 + kk * 64);
    PIN();

    u32x2 ph0[4];

#pragma unroll
    for (int t2 = 0; t2 < 2; ++t2) {
        float sq = 0.f;
#pragma unroll
        for (int kk = 0; kk < 6; ++kk) kf[t2][kk] = scale8(kf[t2][kk], scale, sq);
        sq += __shfl_xor(sq, 16);
        sq += __shfl_xor(sq, 32);
        sq = 0.5f * rb1(sq);
        float hq[4];
#pragma unroll
        for (int r = 0; r < 4; ++r) hq[r] = __shfl(sq, 4 * g4 + r);
#pragma unroll
        for (int mt = 0; mt < 4; ++mt) {
            f32x4 a0 = {0.f, 0.f, 0.f, 0.f};
#pragma unroll
            for (int kk = 0; kk < 6; ++kk) {
                bf16x8 o = ld_frag(omF + ((mt * 6 + kk) * 64 + l) * 16);
                a0 = MFMA16(as_frag(kf[t2][kk]), o, a0);
            }
            u32x2 pf;
            pf[0] = phi2(a0[0], a0[1], hq[0], hq[1]);
            pf[1] = phi2(a0[2], a0[3], hq[2], hq[3]);
            if (t2 == 0) {
                ph0[mt] = pf;
            } else {
                u32x4 full = {ph0[mt][0], ph0[mt][1], pf[0], pf[1]};
                *(u32x4*)(phiF + ((w * 4 + mt) * 64 + l) * 16) = full;
            }
        }
    }

    const char* qbase = (const char*)(Qg + base) + (32 * w + l15) * 384 + g4 * 16;
    u32x4 qf[2][6];
    {
        const int mp = w & 1, dq = w >> 1;
        const int q = l15 >> 2, p = l15 & 3;
        const int rq = 4 * g4 + q;
        const int sw = rq & 6;
        int voff[3];
#pragma unroll
        for (int dt = 0; dt < 3; ++dt) {
            int cidx = 6 * dq + 2 * dt + (p >> 1);
            voff[dt] = rq * 384 + (((cidx & ~7) | ((cidx & 7) ^ sw)) * 16) + (p & 1) * 8;
        }
        f32x4 akv[2][3];
#pragma unroll
        for (int a = 0; a < 2; ++a)
#pragma unroll
            for (int i = 0; i < 3; ++i) akv[a][i] = (f32x4){0.f, 0.f, 0.f, 0.f};
        f32x4 az[2] = {{0.f, 0.f, 0.f, 0.f}, {0.f, 0.f, 0.f, 0.f}};
        bf16x8 ones;
#pragma unroll
        for (int i = 0; i < 8; ++i) ones[i] = (__bf16)1.0f;

#pragma unroll
        for (int c = 0; c < 4; ++c) {
            __syncthreads();
#pragma unroll
            for (int gg = 0; gg < 2; ++gg) {
                const int g = 2 * c + gg;
                bf16x8 a0 = ld_frag(phiF + ((g * 4 + 2 * mp) * 64 + l) * 16);
                bf16x8 a1 = ld_frag(phiF + ((g * 4 + 2 * mp + 1) * 64 + l) * 16);
                const char* vb = Vs + g * (32 * 384);
#pragma unroll
                for (int dt = 0; dt < 3; ++dt) {
                    bf16x8 bb = ld_tr(vb + voff[dt], vb + voff[dt] + 16 * 384);
                    akv[0][dt] = MFMA16(a0, bb, akv[0][dt]);
                    akv[1][dt] = MFMA16(a1, bb, akv[1][dt]);
                }
                az[0] = MFMA16(a0, ones, az[0]);
                az[1] = MFMA16(a1, ones, az[1]);
            }
            PIN();
#pragma unroll
            for (int n = 3 * c; n < 3 * c + 3; ++n) qf[n / 6][n % 6] = ldnt(qbase + (n / 6) * (16 * 384) + (n % 6) * 64);
            PIN();
        }

#pragma unroll
        for (int dt = 0; dt < 3; ++dt) {
            u32x4 cell;
            cell[0] = pk2(akv[0][dt][0], akv[0][dt][1]);
            cell[1] = pk2(akv[0][dt][2], akv[0][dt][3]);
            cell[2] = pk2(akv[1][dt][0], akv[1][dt][1]);
            cell[3] = pk2(akv[1][dt][2], akv[1][dt][3]);
            unsigned off = WS_PART + (unsigned)wg * 24576u + (unsigned)((((3 * dq + dt) * 2 + mp) * 64 + l) * 16);
            __builtin_amdgcn_raw_buffer_store_b128(cell, wsr, off, 0, 16);
        }
        if (dq == 0 && l15 == 0) {
#pragma unroll
            for (int a = 0; a < 2; ++a)
                __builtin_amdgcn_raw_buffer_store_b128(__builtin_bit_cast(u32x4, az[a]), wsr,
                                                       WS_ZPART + (unsigned)wg * 256u + (unsigned)(16 * (2 * mp + a) + 4 * g4) * 4u, 0, 16);
        }
    }
    asm volatile("s_waitcnt vmcnt(0)" ::: "memory");

    __syncthreads();
    if (t == 0) __hip_atomic_store((u64*)(ws + WS_F1 + (unsigned)wg * 64u), tag, __ATOMIC_RELAXED, __HIP_MEMORY_SCOPE_AGENT);

    wait_flags(ws, WS_F1, head, tag, w, l);

    u32x4 sl[8];
    float zv[8];
    if (t < 192) {
#pragma unroll
        for (int c = 0; c < 8; ++c)
            sl[c] = __builtin_amdgcn_raw_buffer_load_b128(
                        wsr, WS_PART + (unsigned)(head * 8 + c) * 24576u + (unsigned)chunk * 3072u + (unsigned)t * 16u, 0, 16);
    } else if (t >= 256 && t < 320) {
#pragma unroll
        for (int c = 0; c < 8; ++c)
            zv[c] = __builtin_bit_cast(float, __builtin_amdgcn_raw_buffer_load_b32(
                        wsr, WS_ZPART + (unsigned)(head * 8 + c) * 256u + (unsigned)(t - 256) * 4u, 0, 16));
    }
    if (t < 192) {
        u32x4 o;
#pragma unroll
        for (int i = 0; i < 4; ++i) {
            float lo = ((bf_lo(sl[0][i]) + bf_lo(sl[1][i])) + (bf_lo(sl[2][i]) + bf_lo(sl[3][i]))) +
                       ((bf_lo(sl[4][i]) + bf_lo(sl[5][i])) + (bf_lo(sl[6][i]) + bf_lo(sl[7][i])));
            float hi = ((bf_hi(sl[0][i]) + bf_hi(sl[1][i])) + (bf_hi(sl[2][i]) + bf_hi(sl[3][i]))) +
                       ((bf_hi(sl[4][i]) + bf_hi(sl[5][i])) + (bf_hi(sl[6][i]) + bf_hi(sl[7][i])));
            o[i] = pk2(lo, hi);
        }
        __builtin_amdgcn_raw_buffer_store_b128(o, wsr, WS_KVF + (unsigned)head * 24576u + (unsigned)(chunk * 192 + t) * 16u, 0, 16);
    } else if (t >= 256 && t < 320) {
        float zs = ((zv[0] + zv[1]) + (zv[2] + zv[3])) + ((zv[4] + zv[5]) + (zv[6] + zv[7]));
        int m = t - 256, ks = m >> 5, mm = m & 31;
        *(uint16_t*)(zF + ks * 64 + ((mm & 15) >> 2) * 16 + (mm >> 4) * 8 + (mm & 3) * 2) = (uint16_t)pk2(zs, zs);
    }
    asm volatile("s_waitcnt vmcnt(0)" ::: "memory");

    __syncthreads();
    if (t == 0) __hip_atomic_store((u64*)(ws + WS_F2 + (unsigned)wg * 64u), tag, __ATOMIC_RELAXED, __HIP_MEMORY_SCOPE_AGENT);

    u32x4 pq[2][2];
#pragma unroll
    for (int t2 = 0; t2 < 2; ++t2) {
        float h;
        {
            float sq = 0.f;
#pragma unroll
            for (int kk = 0; kk < 6; ++kk) qf[t2][kk] = scale8(qf[t2][kk], scale, sq);
            sq += __shfl_xor(sq, 16);
            sq += __shfl_xor(sq, 32);
            h = 0.5f * rb1(sq);
        }
#pragma unroll
        for (int m4 = 0; m4 < 4; ++m4) {
            f32x4 a0 = {0.f, 0.f, 0.f, 0.f};
#pragma unroll
            for (int kk = 0; kk < 6; ++kk) {
                bf16x8 o = ld_frag(omF + ((m4 * 6 + kk) * 64 + l) * 16);
                a0 = MFMA16(o, as_frag(qf[t2][kk]), a0);
            }
            pq[t2][m4 >> 1][(m4 & 1) * 2 + 0] = phi2(a0[0], a0[1], h, h);
            pq[t2][m4 >> 1][(m4 & 1) * 2 + 1] = phi2(a0[2], a0[3], h, h);
        }
    }

    wait_flags(ws, WS_F2, head, tag, w, l);

    {
        u32x4 kvr[3];
#pragma unroll
        for (int i = 0; i < 3; ++i)
            kvr[i] = __builtin_amdgcn_raw_buffer_load_b128(wsr, WS_KVF + (unsigned)head * 24576u + (unsigned)(t + 512 * i) * 16u, 0, 16);
#pragma unroll
        for (int i = 0; i < 3; ++i) *(u32x4*)(kvF + (t + 512 * i) * 16) = kvr[i];
    }
    __syncthreads();

    char* ost = smem + L_OST + w * (16 * 400);
    char* obase = (char*)(Og + base) + (32 * w) * 384;
#pragma unroll
    for (int t2 = 0; t2 < 2; ++t2) {
        float rden;
        {
            bf16x8 z0 = ld_frag(zF + 16 * g4);
            bf16x8 z1 = ld_frag(zF + 64 + 16 * g4);
            f32x4 ad = {0.f, 0.f, 0.f, 0.f};
            ad = MFMA16(z0, as_frag(pq[t2][0]), ad);
            ad = MFMA16(z1, as_frag(pq[t2][1]), ad);
            float den = rb1(rb1(ad[0]) + 1e-6f);
            rden = __builtin_amdgcn_rcpf(den);
        }
#pragma unroll
        for (int dt = 0; dt < 12; ++dt) {
            bf16x8 a0 = ld_frag(kvF + ((dt * 2 + 0) * 64 + l) * 16);
            bf16x8 a1 = ld_frag(kvF + ((dt * 2 + 1) * 64 + l) * 16);
            f32x4 acc = {0.f, 0.f, 0.f, 0.f};
            acc = MFMA16(a0, as_frag(pq[t2][0]), acc);
            acc = MFMA16(a1, as_frag(pq[t2][1]), acc);
            unsigned n01 = pk2(acc[0], acc[1]), n23 = pk2(acc[2], acc[3]);
            u32x2 ov;
            ov[0] = pk2(bf_lo(n01) * rden, bf_hi(n01) * rden);
            ov[1] = pk2(bf_lo(n23) * rden, bf_hi(n23) * rden);
            *(u32x2*)(ost + l15 * 400 + dt * 32 + g4 * 8) = ov;
        }
#pragma unroll
        for (int i = 0; i < 6; ++i) {
            int e = l + 64 * i;
            int r = e / 24, c = e % 24;
            u32x4 v = *(const u32x4*)(ost + r * 400 + c * 16);
            *(u32x4*)(obase + t2 * (16 * 384) + e * 16) = v;
        }
    }
    if (chunk == 0 && t == 0) *(volatile u64*)(ws + WS_EP + (unsigned)head * 64u) = tag;
}

extern "C" void kernel_launch(void* const* d_in, const int* in_sizes, int n_in,
                              void* d_out, int out_size, void* d_ws, size_t ws_size,
                              hipStream_t stream) {
    const uint16_t* q = (const uint16_t*)d_in[0];
    const uint16_t* k = (const uint16_t*)d_in[1];
    const uint16_t* v = (const uint16_t*)d_in[2];
    const uint16_t* om = (const uint16_t*)d_in[3];
    const float scale = (float)pow(192.0, -0.25);
    rfa_fused<<<256, 576, 0, stream>>>(q, k, v, om, (uint16_t*)d_out, (char*)d_ws, scale);
}
```
